```python
import torch

def _softmax(x, axis=-1):
    return torch.softmax(x, dim=axis)
def _gelu(x):
    return torch.nn.functional.gelu(x, approximate="tanh")
def _silu(x):
    return torch.nn.functional.silu(x)
def _sigmoid(x):
    return torch.sigmoid(x)
def _layernorm(x, axis=-1, eps=1e-5):
    m = x.mean(dim=axis, keepdim=True)
    v = x.var(dim=axis, keepdim=True, unbiased=False)
    return (x - m) / torch.sqrt(v + eps)
def _scatter_add(tgt, idx, src, axis=0):
    return tgt.clone().index_add_(axis, idx, src)
def _segment_sum(src, seg, n):
    out = torch.zeros((n,) + src.shape[1:], dtype=src.dtype)
    return out.index_add_(0, seg, src)
def _pad(x, pw):
    flat = tuple(v for pair in reversed(pw) for v in pair)
    return torch.nn.functional.pad(x, flat)

kernel_name = 'gated_mix__sliding_local_dilated__mixer_token_mlp__prompt'

def setup_inputs():

    _w = (lambda a: (lambda t: t.to(torch.bfloat16) if t.is_floating_point() else t)(torch.as_tensor(a)))
    def _A_setup():
        B, S, D = 1, 2048, 512
        return {
            "x": _w((lambda *s: torch.randn(*s, dtype=torch.bfloat16) * 0.05)(B, S, D)),
            "wqkv": _w((lambda *s: torch.randn(*s, dtype=torch.bfloat16) * 0.8)(D, 3 * D)),
            "wo": _w((lambda *s: torch.randn(*s, dtype=torch.bfloat16) * 0.05)(D, D)),
        }
    _A_in = _A_setup()

    def _B_setup():
        B, S, D, F = 1, 2048, 512, 1792
        return {
            "x": _w((lambda *s: torch.randn(*s, dtype=torch.bfloat16) * 0.05)(B, S, D)),
            "w1": _w((lambda *s: torch.randn(*s, dtype=torch.bfloat16) * 1.6)(S, F) * (S ** -0.5)),
            "b1": _w((lambda *s: torch.randn(*s, dtype=torch.bfloat16) * 12.8)(F) * 0.02),
            "w2": _w((lambda *s: torch.randn(*s, dtype=torch.bfloat16) * 0.4)(F, S) * (F ** -0.5)),
            "b2": _w((lambda *s: torch.randn(*s, dtype=torch.bfloat16) * 6.4)(S) * 0.02),
        }
    _B_in = _B_setup()

    return {
        "A_x": _A_in["x"],
        "A_wqkv": _A_in["wqkv"],
        "A_wo": _A_in["wo"],
        "B_x": _B_in["x"],
        "B_w1": _B_in["w1"],
        "B_b1": _B_in["b1"],
        "B_w2": _B_in["w2"],
        "B_b2": _B_in["b2"],
        "gate_w": _w((lambda *s: torch.randn(*s, dtype=torch.bfloat16) * 0.05)(512) * 16.0),
    }

def reference(A_x, A_wqkv, A_wo, B_x, B_w1, B_b1, B_w2, B_b2, gate_w):
    def _A_ref(x, wqkv, wo):
        B, S, D = x.shape
        H = 32; Dh = D // H
        W, dil = 8, 2
        qkv = (lambda x, *d: x.permute(*d))((x @ wqkv).reshape(B, S, 3, H, Dh), 2, 0, 3, 1, 4)
        q, k, v = qkv[0], qkv[1], qkv[2]
        scale = Dh ** -0.5
        pos = torch.arange(S)
        off = torch.arange(W) * dil
        idx = pos[:, None] - off[None, :]
        mask = idx >= 0
        idx_c = torch.where(mask, idx, 0).reshape(-1)
        kg = k[:, :, idx_c, :].reshape(B, H, S, W, Dh)
        vg = v[:, :, idx_c, :].reshape(B, H, S, W, Dh)
        logits = torch.einsum("bhsd,bhswd->bhsw", q, kg) * scale
        logits = torch.where(mask[None, None, :, :], logits, -1e9)
        attn = _softmax(logits, axis=-1)
        o = torch.einsum("bhsw,bhswd->bhsd", attn, vg)
        return (lambda x, *d: x.permute(*d))(o, 0, 2, 1, 3).reshape(B, S, D) @ wo

    def _B_ref(x, w1, b1, w2, b2):
        B, S, D = x.shape
        h = _layernorm(x, axis=-1)
        u = torch.einsum("bsd,sf->bdf", h, w1) + b1
        u = _gelu(u)
        y = torch.einsum("bdf,fs->bds", u, w2) + b2
        return (lambda x, *d: x.permute(*d))(y, 0, 2, 1)

    x = A_x
    g = _sigmoid((x * gate_w).sum(-1, keepdims=True))
    a = _A_ref(x, A_wqkv, A_wo)
    b = _B_ref(x, B_w1, B_b1, B_w2, B_b2)
    return g * a + (1 - g) * b
```

```cpp
#include <hip/hip_runtime.h>
#include <hip/hip_bf16.h>
#include <stdint.h>

typedef __attribute__((ext_vector_type(8))) __bf16 bf16x8;
typedef __attribute__((ext_vector_type(2))) __bf16 bf16x2;
typedef __attribute__((ext_vector_type(4))) float f32x4;
typedef __attribute__((ext_vector_type(4))) unsigned u32x4;
typedef __attribute__((ext_vector_type(2))) unsigned u32x2;
typedef __amdgpu_buffer_rsrc_t rsrc_t;

#define DEV __device__ __forceinline__
#define NBUF 2
#define PF1 0
#define PF2 0

constexpr int S_ = 2048, D_ = 512, F_ = 1792;
constexpr size_t WS_HT = 0;
constexpr size_t WS_UG = 2u << 20;
constexpr size_t WS_O = 4u << 20;
constexpr size_t WS_G = 6u << 20;

DEV float rbf(float x) { return (float)(__bf16)x; }
DEV unsigned pack2(float lo, float hi) {
  bf16x2 v; v[0] = (__bf16)lo; v[1] = (__bf16)hi; return __builtin_bit_cast(unsigned, v);
}
DEV float bflo(unsigned u) { return __builtin_bit_cast(float, u << 16); }
DEV float bfhi(unsigned u) { return __builtin_bit_cast(float, u & 0xffff0000u); }
DEV float dot2(unsigned a, unsigned b, float c) {
  return __builtin_amdgcn_fdot2_f32_bf16(__builtin_bit_cast(bf16x2, a), __builtin_bit_cast(bf16x2, b), c, false);
}
DEV rsrc_t mk_rsrc(const void* p, unsigned bytes) {
  return __builtin_amdgcn_make_buffer_rsrc(const_cast<void*>(p), 0, (int)bytes, 0x00020000);
}
DEV f32x4 mfma(bf16x8 a, bf16x8 b, f32x4 c) { return __builtin_amdgcn_mfma_f32_16x16x32_bf16(a, b, c, 0, 0, 0); }

DEV void wfrag(const u32x4 (&R)[8], bf16x8 (&WA)[4], bf16x8 (&WB)[4]) {
#pragma unroll
  for (int m = 0; m < 4; ++m) {
    u32x4 a, b;
#pragma unroll
    for (int t = 0; t < 4; ++t) {
      const unsigned sel = (m & 1) ? 0x07060302u : 0x05040100u;
      const unsigned lo = __builtin_amdgcn_perm(R[2 * t + 1][m >> 1], R[2 * t][m >> 1], sel);
      const unsigned hi = __builtin_amdgcn_perm(R[2 * t + 1][2 + (m >> 1)], R[2 * t][2 + (m >> 1)], sel);
      a[t] = __builtin_amdgcn_update_dpp(lo, hi, 0x128, 0xf, 0xc, false);
      b[t] = __builtin_amdgcn_update_dpp(hi, lo, 0x128, 0xf, 0x3, false);
    }
    WA[m] = __builtin_bit_cast(bf16x8, a);
    WB[m] = __builtin_bit_cast(bf16x8, b);
  }
}

#define FENCE() __builtin_amdgcn_sched_barrier(0)
template <int NI>
DEV void prefetch_rows(const char* base, unsigned row_stride, unsigned chunks_per_row, unsigned first_chunk, int lane) {
  u32x4 v[8];
#pragma unroll 1
  for (int k0 = 0; k0 < NI; k0 += 8) {
#pragma unroll
    for (int k = 0; k < 8; ++k) {
      const unsigned c = first_chunk + (unsigned)(k0 + k) * 64u + (unsigned)lane;
      const unsigned row = c / chunks_per_row, ch = c - row * chunks_per_row;
      v[k] = *(const u32x4*)(base + (size_t)row * row_stride + ch * 16u);
    }
#pragma unroll
    for (int k = 0; k < 8; ++k) asm volatile("" ::"v"(v[k]));
  }
}

__global__ __launch_bounds__(256 + 64 * PF1, 1) void k1_qkv_attn(const __bf16* __restrict__ x, const __bf16* __restrict__ wqkv,
                                                      const __bf16* __restrict__ gw, const __bf16* __restrict__ w1,
                                                      char* __restrict__ ws) {
  extern __shared__ __attribute__((aligned(16))) char smem[];
  const int tid = threadIdx.x, lane = tid & 63;
  const int wid = __builtin_amdgcn_readfirstlane(tid >> 6);
  const int xcd = blockIdx.x & 7, jj = blockIdx.x >> 3;
  const int st = (xcd >> 1) * 8 + (jj >> 2), hg = (xcd & 1) * 4 + (jj & 3);
  if (wid == 4) {
    return;
  }
  const int s0 = st * 64;
  const int g = lane >> 4, i = lane & 15;

#pragma unroll
  for (int r = 0; r < 20; ++r) {
    const int row = wid * 20 + r;
    int sr = s0 - 16 + row; sr = sr < 0 ? 0 : sr;
    const __bf16* src = x + (size_t)sr * D_ + ((lane ^ (row & 15)) << 3);
    __builtin_amdgcn_global_load_lds((const __attribute__((address_space(1))) void*)src,
                                     (__attribute__((address_space(3))) void*)(smem + row * 1024 + lane * 16), 16, 0, 0);
  }

  f32x4 acc[4][5];
#pragma unroll
  for (int a = 0; a < 4; ++a)
#pragma unroll
    for (int b = 0; b < 5; ++b) acc[a][b] = f32x4{0.f, 0.f, 0.f, 0.f};

  if (wid < 3) {
    const rsrc_t W = mk_rsrc(wqkv, 512u * 1536u * 2u);
    const unsigned wv = (unsigned)((32 * (i >> 3) + 8 * g) * 1536 + 8 * (i & 7)) * 2u;
    const unsigned ws0 = (unsigned)(wid * 512 + hg * 64) * 2u;
    constexpr int NB = 3;
    u32x4 R[NB][8];
    auto issue = [&](int p) {
#pragma unroll
      for (int j = 0; j < 8; ++j)
        R[p % NB][j] = __builtin_amdgcn_raw_buffer_load_b128(W, wv, ws0 + (unsigned)(64 * p + j) * 3072u, 0);
    };
#pragma unroll
    for (int p = 0; p < NB - 1; ++p) issue(p);
    __syncthreads();
    const char* xbase = smem + i * 1024;
#pragma unroll
    for (int p = 0; p < 8; ++p) {
      if (p + NB - 1 < 8) issue(p + NB - 1);
      bf16x8 Wf[2][4];
      wfrag(R[p % NB], Wf[0], Wf[1]);
#pragma unroll
      for (int h = 0; h < 2; ++h) {
        bf16x8 Xf[5];
#pragma unroll
        for (int mt = 0; mt < 5; ++mt)
          Xf[mt] = *(const bf16x8*)(xbase + mt * 16384 + (((4 * (2 * p + h) + g) ^ i) << 4));
#pragma unroll
        for (int q = 0; q < 4; ++q)
#pragma unroll
          for (int mt = 0; mt < 5; ++mt) {
            if (mt == 0 && wid == 0) continue;
            acc[q][mt] = mfma(Wf[h][q], Xf[mt], acc[q][mt]);
          }
      }
    }
  } else {
    const u32x4 gwv = *(const u32x4*)(gw + lane * 8);
    __syncthreads();
    float xs[8][8], sum[8], gs[8];
#pragma unroll
    for (int r = 0; r < 8; ++r) {
      const int row = 16 + 8 * hg + r;
      const u32x4 v = *(const u32x4*)(smem + row * 1024 + ((lane ^ (row & 15)) << 4));
      sum[r] = 0.f; gs[r] = 0.f;
#pragma unroll
      for (int e = 0; e < 4; ++e) {
        xs[r][2 * e] = bflo(v[e]); xs[r][2 * e + 1] = bfhi(v[e]);
        gs[r] += rbf(xs[r][2 * e] * bflo(gwv[e])) + rbf(xs[r][2 * e + 1] * bfhi(gwv[e]));
        sum[r] += xs[r][2 * e] + xs[r][2 * e + 1];
      }
    }
#pragma unroll
    for (int o = 32; o > 0; o >>= 1)
#pragma unroll
      for (int r = 0; r < 8; ++r) { sum[r] += __shfl_xor(sum[r], o); gs[r] += __shfl_xor(gs[r], o); }
    float sq[8];
#pragma unroll
    for (int r = 0; r < 8; ++r) {
      sum[r] *= (1.f / 512.f);
      sq[r] = 0.f;
#pragma unroll
      for (int e = 0; e < 8; ++e) { const float d = xs[r][e] - sum[r]; sq[r] += d * d; }
    }
#pragma unroll
    for (int o = 32; o > 0; o >>= 1)
#pragma unroll
      for (int r = 0; r < 8; ++r) sq[r] += __shfl_xor(sq[r], o);
    float gsel = 0.f;
#pragma unroll
    for (int r = 0; r < 8; ++r) {
      const float mb = rbf(sum[r]);
      const float vb = rbf(sq[r] * (1.f / 512.f));
      const float den = rbf(sqrtf(rbf(vb + 1e-5f)));
      const float rden = 1.f / den;
#pragma unroll
      for (int e = 0; e < 8; ++e) xs[r][e] = rbf(xs[r][e] - mb) * rden;
      gsel = (lane == r) ? gs[r] : gsel;
    }
    if (lane < 8) ((float*)(ws + WS_G))[s0 + 8 * hg + lane] = rbf(1.f / (1.f + __expf(-rbf(gsel))));
    const int s8 = s0 + 8 * hg;
    const int ks = s8 >> 5, gg = (s8 >> 3) & 3;
#pragma unroll
    for (int e = 0; e < 8; ++e) {
      const int d = lane * 8 + e;
      u32x4 o;
#pragma unroll
      for (int m = 0; m < 4; ++m) o[m] = pack2(xs[2 * m][e], xs[2 * m + 1][e]);
      *(u32x4*)(ws + WS_HT + (size_t)(((d >> 4) * 64 + ks) * 1024 + (gg * 16 + (d & 15)) * 16)) = o;
    }
  }
  __syncthreads();
  if (wid < 3) {
#pragma unroll
    for (int mt = 0; mt < 5; ++mt) {
      const int row = 16 * mt + i;
#pragma unroll
      for (int r = 0; r < 4; ++r) {
        u32x2 o;
        o[0] = pack2(acc[0][mt][r], acc[1][mt][r]);
        o[1] = pack2(acc[2][mt][r], acc[3][mt][r]);
        *(u32x2*)(smem + (((wid * 2 + (r & 1)) * 4 + 2 * (g & 1) + (r >> 1)) * 80 + row) * 16 + 8 * (g >> 1)) = o;
      }
    }
  }
  __syncthreads();
  {
    const int sl = 16 * wid + i;
    const int s = s0 + sl;
    const int row = sl + 16;
    auto ld = [&](int sec, int hf, int r) { return *(const u32x4*)(smem + (((sec * 2 + hf) * 4 + g) * 80 + r) * 16); };
    const u32x4 q0 = ld(0, 0, row), q1 = ld(0, 1, row);
    float lg[8];
    float mx = -3.0e38f;
#pragma unroll
    for (int w = 0; w < 8; ++w) {
      const u32x4 k0 = ld(1, 0, row - 2 * w), k1 = ld(1, 1, row - 2 * w);
      float d = 0.f;
#pragma unroll
      for (int e = 0; e < 4; ++e) {
        d = dot2(q0[e], k0[e], d);
        d = dot2(q1[e], k1[e], d);
      }
      float l = rbf(rbf(d) * 0.25f);
      l = (s - 2 * w >= 0) ? l : -1e9f;
      lg[w] = l;
      mx = fmaxf(mx, l);
    }
    float sum = 0.f;
#pragma unroll
    for (int w = 0; w < 8; ++w) { lg[w] = __expf(lg[w] - mx); sum += lg[w]; }
    const float inv = 1.f / sum;
    float o[16];
#pragma unroll
    for (int e = 0; e < 16; ++e) o[e] = 0.f;
#pragma unroll
    for (int w = 0; w < 8; ++w) {
      const float p = rbf(lg[w] * inv);
      const u32x4 v0 = ld(2, 0, row - 2 * w), v1 = ld(2, 1, row - 2 * w);
#pragma unroll
      for (int e = 0; e < 4; ++e) {
        o[2 * e] += p * bflo(v0[e]); o[2 * e + 1] += p * bfhi(v0[e]);
        o[8 + 2 * e] += p * bflo(v1[e]); o[8 + 2 * e + 1] += p * bfhi(v1[e]);
      }
    }
    const int h = hg * 4 + g;
    char* dst = ws + WS_O + (size_t)(((st * 4 + wid) * 16 + (h >> 1)) * 1024 + ((2 * (h & 1)) * 16 + i) * 16);
    u32x4 a, b;
#pragma unroll
    for (int m = 0; m < 4; ++m) { a[m] = pack2(o[2 * m], o[2 * m + 1]); b[m] = pack2(o[8 + 2 * m], o[8 + 2 * m + 1]); }
    *(u32x4*)dst = a;
    *(u32x4*)(dst + 256) = b;
  }
}

#define LDSP __attribute__((address_space(3)))
typedef __attribute__((ext_vector_type(4))) __bf16 bf16x4;
#define NSLOT 6
constexpr int NS = NSLOT;
constexpr int PD = NS - 1;
DEV void dma16(rsrc_t r, LDSP char* dst, unsigned voff, unsigned soff) {
  __builtin_amdgcn_raw_ptr_buffer_load_lds(r, dst, 16, voff, soff, 0, 0);
}
template <int N> DEV void wait_vm() { asm volatile("s_waitcnt vmcnt(%0)" ::"n"(N) : "memory"); }
DEV void bar() {
  __builtin_amdgcn_sched_barrier(0);
  asm volatile("s_waitcnt lgkmcnt(0)" ::: "memory"); __builtin_amdgcn_s_barrier(); asm volatile("" ::: "memory");
  __builtin_amdgcn_sched_barrier(0);
}
DEV bf16x8 tr8(LDSP char* p) {
  const bf16x4 a = __builtin_amdgcn_ds_read_tr16_b64_v4bf16((LDSP bf16x4*)p);
  const bf16x4 b = __builtin_amdgcn_ds_read_tr16_b64_v4bf16((LDSP bf16x4*)(p + 512));
  return __builtin_shufflevector(a, b, 0, 1, 2, 3, 4, 5, 6, 7);
}
template <int R> DEV void tail_waits() { wait_vm<4 * R>(); bar(); if constexpr (R > 0) tail_waits<R - 1>(); }
template <int NSTEP, class F>
DEV void loader_loop(F issue) {
#pragma unroll
  for (int s = 0; s < PD; ++s) issue(s, s);
  int slot = PD % NS;
#pragma unroll 1
  for (int s = 0; s < NSTEP - PD; ++s) {
    wait_vm<4 * (PD - 1)>();
    bar();
    issue(s + PD, slot);
    slot = slot + 1 == NS ? 0 : slot + 1;
  }
  tail_waits<PD - 1>();
}
struct Frags { bf16x8 w[2][2], p[2][2]; };
DEV void read_frags(Frags& f, LDSP char* slot, unsigned wo0, unsigned wo1, unsigned po) {
#pragma unroll
  for (int h = 0; h < 2; ++h) {
    f.w[h][0] = tr8(slot + wo0 + 4096 * h);
    f.p[h][0] = *(LDSP bf16x8*)(slot + po + 4096 * h);
    f.w[h][1] = tr8(slot + wo1 + 4096 * h);
    f.p[h][1] = *(LDSP bf16x8*)(slot + po + 4096 * h + 1024);
  }
  __builtin_amdgcn_sched_barrier(0);
}
template <bool W_IS_A>
DEV void mma_frags(f32x4 (&acc)[2][2], const Frags& f) {
#pragma unroll
  for (int h = 0; h < 2; ++h)
#pragma unroll
    for (int a = 0; a < 2; ++a)
#pragma unroll
      for (int b = 0; b < 2; ++b)
        acc[a][b] = W_IS_A ? mfma(f.w[h][a], f.p[h][b], acc[a][b]) : mfma(f.p[h][a], f.w[h][b], acc[a][b]);
}
DEV unsigned w_lane_off(int n, int g, int i) {
  const int q = i >> 2, p = i & 3;
  const int swz = ((q >> 1) & 1) | ((g & 1) << 1);
  return (unsigned)((8 * g + q) * 128 + (((n ^ swz) * 2 + (p >> 1)) * 16) + 8 * (p & 1));
}
DEV unsigned w_src_off(int lane, int par, unsigned stride) {
  const int swz = ((lane >> 4) & 1) | (par << 1);
  return (unsigned)(lane >> 3) * stride + (unsigned)(((lane & 7) ^ (swz << 1)) * 16);
}

__global__ __launch_bounds__(512, 1) void k2_mlp1(const __bf16* __restrict__ w1, const __bf16* __restrict__ b1,
                                                  const __bf16* __restrict__ w2, const __bf16* __restrict__ wo,
                                                  char* __restrict__ ws) {
  extern __shared__ __attribute__((aligned(16))) char smem_g[];
  LDSP char* smem = (LDSP char*)smem_g;
  const int tid = threadIdx.x, lane = tid & 63;
  const int wid = __builtin_amdgcn_readfirstlane(tid >> 6);
  const int xcd = blockIdx.x & 7, jj = blockIdx.x >> 3;
  if (jj >= 28) {
    return;
  }
  const int ft = (xcd >> 1) * 7 + (jj >> 2), dt = (xcd & 1) * 4 + (jj & 3);
  constexpr int NSTEP = 32;
  if (wid >= 4) {
    const int hh = wid & 1;
    if (wid < 6) {
      const rsrc_t W = mk_rsrc(w1, (unsigned)S_ * F_ * 2u);
      const unsigned v0 = w_src_off(lane, 0, F_ * 2u), v1 = w_src_off(lane, 1, F_ * 2u);
      loader_loop<NSTEP>([&](int s, int slot) {
#pragma unroll
        for (int j = 0; j < 4; ++j)
          dma16(W, smem + slot * 16384 + hh * 4096 + j * 1024, (j & 1) ? v1 : v0, (unsigned)(ft * 128) + (unsigned)(64 * s + 32 * hh + 8 * j) * (F_ * 2u));
      });
    } else {
      const rsrc_t P = mk_rsrc(ws + WS_HT, 2u << 20);
      loader_loop<NSTEP>([&](int s, int slot) {
#pragma unroll
        for (int j = 0; j < 4; ++j)
          dma16(P, smem + slot * 16384 + 8192 + hh * 4096 + j * 1024, lane * 16u, (unsigned)((dt * 4 + j) * 64 + 2 * s + hh) * 1024u);
      });
    }
    return;
  }
  const int g = lane >> 4, i = lane & 15;
  const int ww = wid & 1, wp = wid >> 1;
  u32x2 bv[2];
#pragma unroll
  for (int a = 0; a < 2; ++a) bv[a] = *(const u32x2*)(b1 + ft * 64 + 32 * ww + 16 * a + 4 * g);
  const unsigned wo0 = w_lane_off(2 * ww, g, i), wo1 = w_lane_off(2 * ww + 1, g, i), po = 8192u + (unsigned)(2 * wp) * 1024u + lane * 16u;
  f32x4 acc[2][2];
#pragma unroll
  for (int a = 0; a < 2; ++a)
#pragma unroll
    for (int b = 0; b < 2; ++b) acc[a][b] = f32x4{0.f, 0.f, 0.f, 0.f};
  Frags fa, fb;
  bar();
  read_frags(fa, smem, wo0, wo1, po);
  int slot = 1;
#pragma unroll 1
  for (int s = 1; s < NSTEP - 1; s += 2) {
    bar();
    read_frags(fb, smem + slot * 16384, wo0, wo1, po);
    slot = slot + 1 == NS ? 0 : slot + 1;
    mma_frags<true>(acc, fa);
    bar();
    read_frags(fa, smem + slot * 16384, wo0, wo1, po);
    slot = slot + 1 == NS ? 0 : slot + 1;
    mma_frags<true>(acc, fb);
  }
  bar();
  read_frags(fb, smem + slot * 16384, wo0, wo1, po);
  mma_frags<true>(acc, fa);
  mma_frags<true>(acc, fb);
#pragma unroll
  for (int a = 0; a < 2; ++a)
#pragma unroll
    for (int b = 0; b < 2; ++b) {
      float r4[4];
#pragma unroll
      for (int r = 0; r < 4; ++r) {
        const unsigned bw = bv[a][r >> 1];
        const float bb = (r & 1) ? bfhi(bw) : bflo(bw);
        const float u = rbf(rbf(acc[a][b][r]) + bb);
        const float y = 0.7978845608028654f * (u + 0.044715f * u * u * u);
        const float t = 1.f - 2.f * __builtin_amdgcn_rcpf(__expf(2.f * y) + 1.f);
        r4[r] = 0.5f * u * (1.f + t);
      }
      u32x2 o;
      o[0] = pack2(r4[0], r4[1]); o[1] = pack2(r4[2], r4[3]);
      *(u32x2*)(ws + WS_UG + (size_t)(((dt * 4 + 2 * wp + b) * 56 + 2 * ft + ww) * 1024 + ((2 * a + (g >> 1)) * 16 + i) * 16 + 8 * (g & 1))) = o;
    }
}

__global__ __launch_bounds__(512, 1) void k3_out(const __bf16* __restrict__ wo, const __bf16* __restrict__ w2,
                                                 const __bf16* __restrict__ b2, const char* __restrict__ ws,
                                                 __bf16* __restrict__ out) {
  extern __shared__ __attribute__((aligned(16))) char smem_g[];
  LDSP char* smem = (LDSP char*)smem_g;
  const int tid = threadIdx.x, lane = tid & 63;
  const int wid = __builtin_amdgcn_readfirstlane(tid >> 6);
  const int xcd = blockIdx.x & 7, jj = blockIdx.x >> 3;
  const int st = (xcd >> 1) * 8 + (jj >> 2), dt = (xcd & 1) * 4 + (jj & 3);
  constexpr int NSTEP = 36;
  if (wid >= 4) {
    const int hh = wid & 1;
    if (wid < 6) {
      const rsrc_t Wa = mk_rsrc(wo, 512u * 512u * 2u);
      const rsrc_t Wb = mk_rsrc(w2, (unsigned)S_ * F_ * 2u);
      const unsigned a0 = w_src_off(lane, 0, D_ * 2u), a1 = w_src_off(lane, 1, D_ * 2u);
      const unsigned c0 = w_src_off(lane, 0, S_ * 2u), c1 = w_src_off(lane, 1, S_ * 2u);
      loader_loop<NSTEP>([&](int s, int slot) {
        if (s < 8) {
#pragma unroll
          for (int j = 0; j < 4; ++j)
            dma16(Wa, smem + slot * 16384 + hh * 4096 + j * 1024, (j & 1) ? a1 : a0, (unsigned)(dt * 128) + (unsigned)(64 * s + 32 * hh + 8 * j) * (D_ * 2u));
        } else {
#pragma unroll
          for (int j = 0; j < 4; ++j)
            dma16(Wb, smem + slot * 16384 + hh * 4096 + j * 1024, (j & 1) ? c1 : c0, (unsigned)(st * 128) + (unsigned)(64 * (s - 8) + 32 * hh + 8 * j) * (S_ * 2u));
        }
      });
    } else {
      const rsrc_t P = mk_rsrc(ws, 8u << 20);
      loader_loop<NSTEP>([&](int s, int slot) {
        const unsigned base = s < 8 ? (unsigned)WS_O + (unsigned)(st * 4 * 16 + 2 * s + hh) * 1024u : (unsigned)WS_UG + (unsigned)(dt * 4 * 56 + 2 * (s - 8) + hh) * 1024u;
        const unsigned tq = s < 8 ? 16u * 1024u : 56u * 1024u;
#pragma unroll
        for (int j = 0; j < 4; ++j)
          dma16(P, smem + slot * 16384 + 8192 + hh * 4096 + j * 1024, lane * 16u, base + (unsigned)j * tq);
      });
    }
    return;
  }
  const int g = lane >> 4, i = lane & 15;
  const int X = wid & 1, Y = wid >> 1;
  float gt[2], bb[2];
#pragma unroll
  for (int b = 0; b < 2; ++b) {
    const int s = st * 64 + 32 * Y + 16 * b + i;
    gt[b] = ((const float*)(ws + WS_G))[s];
    bb[b] = (float)b2[s];
  }
  f32x4 aa[2][2], ab[2][2];
#pragma unroll
  for (int a = 0; a < 2; ++a)
#pragma unroll
    for (int b = 0; b < 2; ++b) { aa[a][b] = f32x4{0.f, 0.f, 0.f, 0.f}; ab[a][b] = f32x4{0.f, 0.f, 0.f, 0.f}; }
  const unsigned wa0 = w_lane_off(2 * X, g, i), wa1 = w_lane_off(2 * X + 1, g, i), pa = 8192u + (unsigned)(2 * Y) * 1024u + lane * 16u;
  const unsigned wb0 = w_lane_off(2 * Y, g, i), wb1 = w_lane_off(2 * Y + 1, g, i), pb = 8192u + (unsigned)(2 * X) * 1024u + lane * 16u;
  Frags fa, fb;
  bar();
  read_frags(fa, smem, wa0, wa1, pa);
  int slot = 1;
#pragma unroll 1
  for (int s = 1; s < 7; s += 2) {
    bar();
    read_frags(fb, smem + slot * 16384, wa0, wa1, pa);
    slot = slot + 1 == NS ? 0 : slot + 1;
    mma_frags<true>(aa, fa);
    bar();
    read_frags(fa, smem + slot * 16384, wa0, wa1, pa);
    slot = slot + 1 == NS ? 0 : slot + 1;
    mma_frags<true>(aa, fb);
  }
  bar();
  read_frags(fb, smem + slot * 16384, wa0, wa1, pa);
  slot = slot + 1 == NS ? 0 : slot + 1;
  mma_frags<true>(aa, fa);
  bar();
  read_frags(fa, smem + slot * 16384, wb0, wb1, pb);
  slot = slot + 1 == NS ? 0 : slot + 1;
  mma_frags<true>(aa, fb);
#pragma unroll 1
  for (int s = 9; s < NSTEP - 1; s += 2) {
    bar();
    read_frags(fb, smem + slot * 16384, wb0, wb1, pb);
    slot = slot + 1 == NS ? 0 : slot + 1;
    mma_frags<false>(ab, fa);
    bar();
    read_frags(fa, smem + slot * 16384, wb0, wb1, pb);
    slot = slot + 1 == NS ? 0 : slot + 1;
    mma_frags<false>(ab, fb);
  }
  bar();
  read_frags(fb, smem + slot * 16384, wb0, wb1, pb);
  mma_frags<false>(ab, fa);
  mma_frags<false>(ab, fb);
#pragma unroll
  for (int b = 0; b < 2; ++b) {
    const float omg = rbf(1.f - gt[b]);
#pragma unroll
    for (int a = 0; a < 2; ++a) {
      float r4[4];
#pragma unroll
      for (int r = 0; r < 4; ++r) {
        const float av = rbf(aa[a][b][r]);
        const float bv = rbf(rbf(ab[a][b][r]) + bb[b]);
        r4[r] = rbf(gt[b] * av) + rbf(omg * bv);
      }
      u32x2 o;
      o[0] = pack2(r4[0], r4[1]); o[1] = pack2(r4[2], r4[3]);
      *(u32x2*)(out + (size_t)(st * 64 + 32 * Y + 16 * b + i) * D_ + dt * 64 + 32 * X + 16 * a + 4 * g) = o;
    }
  }
}

extern "C" void kernel_launch(void* const* d_in, const int* in_sizes, int n_in, void* d_out, int out_size,
                              void* d_ws, size_t ws_size, hipStream_t stream) {
  const __bf16* x = (const __bf16*)d_in[0];
  const __bf16* wqkv = (const __bf16*)d_in[1];
  const __bf16* wo = (const __bf16*)d_in[2];
  const __bf16* w1 = (const __bf16*)d_in[4];
  const __bf16* b1 = (const __bf16*)d_in[5];
  const __bf16* w2 = (const __bf16*)d_in[6];
  const __bf16* b2 = (const __bf16*)d_in[7];
  const __bf16* gw = (const __bf16*)d_in[8];
  char* ws = (char*)d_ws;
  (void)hipFuncSetAttribute((const void*)k1_qkv_attn, hipFuncAttributeMaxDynamicSharedMemorySize, 81920);
  (void)hipFuncSetAttribute((const void*)k2_mlp1, hipFuncAttributeMaxDynamicSharedMemorySize, NS * 16384);
  (void)hipFuncSetAttribute((const void*)k3_out, hipFuncAttributeMaxDynamicSharedMemorySize, NS * 16384);
  k1_qkv_attn<<<256, 256 + 64 * PF1, 81920, stream>>>(x, wqkv, gw, w1, ws);
  k2_mlp1<<<256, 512, NS * 16384, stream>>>(w1, b1, w2, wo, ws);
  k3_out<<<256, 512, NS * 16384, stream>>>(wo, w2, b2, ws, (__bf16*)d_out);
}
```

```cpp
#include <hip/hip_runtime.h>
#include <hip/hip_bf16.h>
#include <stdint.h>
#include <type_traits>

typedef __attribute__((ext_vector_type(8))) __bf16 bf16x8;
typedef __attribute__((ext_vector_type(4))) __bf16 bf16x4;
typedef __attribute__((ext_vector_type(2))) __bf16 bf16x2;
typedef __attribute__((ext_vector_type(4))) float f32x4;
typedef __attribute__((ext_vector_type(4))) unsigned u32x4;
typedef __attribute__((ext_vector_type(2))) unsigned u32x2;
typedef __amdgpu_buffer_rsrc_t rsrc_t;

#define DEV __device__ __forceinline__
#define LDSP __attribute__((address_space(3)))
#define FENCE() __builtin_amdgcn_sched_barrier(0)
#define DIAGK 0
#define RB 1
#define RQ 1
#define RM 1
#define PE 0
#define PFW2 1

constexpr int S_ = 2048, D_ = 512, F_ = 1792;
constexpr size_t WS_HT = 0;
constexpr size_t WS_UG = 2u << 20;
constexpr size_t WS_O = 4u << 20;
constexpr size_t WS_G = 6u << 20;
constexpr size_t WS_F = (6u << 20) + 65536;
constexpr size_t WS_F2 = WS_F + 4096;
constexpr size_t WS_F3 = WS_F + 8192;
constexpr size_t WS_E = (6u << 20) + 131072;
constexpr unsigned TAGC = 0x9E3779B9u;
constexpr int SC1 = 16;

DEV float rbf(float x) { return (float)(__bf16)x; }
DEV unsigned pack2(float lo, float hi) {
  bf16x2 v; v[0] = (__bf16)lo; v[1] = (__bf16)hi; return __builtin_bit_cast(unsigned, v);
}
DEV float bflo(unsigned u) { return __builtin_bit_cast(float, u << 16); }
DEV float bfhi(unsigned u) { return __builtin_bit_cast(float, u & 0xffff0000u); }
DEV float dot2(unsigned a, unsigned b, float c) {
  return __builtin_amdgcn_fdot2_f32_bf16(__builtin_bit_cast(bf16x2, a), __builtin_bit_cast(bf16x2, b), c, false);
}
DEV rsrc_t mk_rsrc(const void* p, unsigned bytes) {
  return __builtin_amdgcn_make_buffer_rsrc(const_cast<void*>(p), 0, (int)bytes, 0x00020000);
}
DEV f32x4 mfma(bf16x8 a, bf16x8 b, f32x4 c) { return __builtin_amdgcn_mfma_f32_16x16x32_bf16(a, b, c, 0, 0, 0); }

template <int NI, unsigned GPR>
DEV void touch(unsigned (&v)[NI], rsrc_t r, unsigned soff, unsigned row_stride, unsigned limit, int lane) {
#pragma unroll
  for (int k = 0; k < NI; ++k) {
    const unsigned c = (unsigned)k * 64u + (unsigned)lane;
    const unsigned row = c / GPR, col = c - row * GPR;
    const unsigned voff = c < limit ? row * row_stride + col * 128u : 0xfffffff0u;
    v[k] = __builtin_amdgcn_raw_buffer_load_b32(r, voff, soff, 0);
  }
}
template <int NI> DEV void sink(const unsigned (&v)[NI]) {
#pragma unroll
  for (int k = 0; k < NI; ++k) asm volatile("" ::"v"(v[k]));
}
DEV float fbits(unsigned u) { return __builtin_bit_cast(float, u); }
DEV unsigned ubits(float f) { return __builtin_bit_cast(unsigned, f); }
template <int CTRL> DEV float dpp_add(float v) {
  return v + fbits((unsigned)__builtin_amdgcn_update_dpp(0, (int)ubits(v), CTRL, 0xf, 0xf, false));
}
DEV float wave_sum(float v) {
  v = dpp_add<0x128>(v); v = dpp_add<0x124>(v); v = dpp_add<0x122>(v); v = dpp_add<0x121>(v);
  { const auto r = __builtin_amdgcn_permlane16_swap(ubits(v), ubits(v), false, false); v = fbits(r[0]) + fbits(r[1]); }
  { const auto r = __builtin_amdgcn_permlane32_swap(ubits(v), ubits(v), false, false); v = fbits(r[0]) + fbits(r[1]); }
  return v;
}

constexpr int NS = 4;
constexpr int SLOT = 34816;
constexpr int AX = NS * SLOT;
constexpr int LDS_BYTES = 163840;
DEV void dma16(rsrc_t r, LDSP char* dst, unsigned voff, unsigned soff) {
  __builtin_amdgcn_raw_ptr_buffer_load_lds(r, dst, 16, voff, soff, 0, 0);
}
template <int N> DEV void wait_vm() { asm volatile("s_waitcnt vmcnt(%0)" ::"n"(N) : "memory"); }
DEV void bar() {
  FENCE();
  asm volatile("s_waitcnt lgkmcnt(0)" ::: "memory"); __builtin_amdgcn_s_barrier(); asm volatile("" ::: "memory");
  FENCE();
}
DEV bf16x8 tr8(LDSP char* p) {
  const bf16x4 a = __builtin_amdgcn_ds_read_tr16_b64_v4bf16((LDSP bf16x4*)p);
  const bf16x4 b = __builtin_amdgcn_ds_read_tr16_b64_v4bf16((LDSP bf16x4*)(p + 512));
  return __builtin_shufflevector(a, b, 0, 1, 2, 3, 4, 5, 6, 7);
}
DEV unsigned w_lane_off(int n, int g, int i) {
  const int q = i >> 2, p = i & 3;
  const int swz = ((q >> 1) & 1) | ((g & 1) << 1);
  return (unsigned)((8 * g + q) * 128 + (((n ^ swz) * 2 + (p >> 1)) * 16) + 8 * (p & 1));
}
DEV unsigned w_src_off(int lane, int par, unsigned stride) {
  const int swz = ((lane >> 4) & 1) | (par << 1);
  return (unsigned)(lane >> 3) * stride + (unsigned)(((lane & 7) ^ (swz << 1)) * 16);
}
#define STAMP(k)
#define DIAG_INIT
#define DIAG_DUMP(cond)
#define DIAG_ARGS
#define DIAG_PASS
constexpr int WD = 3;
template <int OFF> DEV u32x2 tr_asm(unsigned addr) {
  u32x2 r;
  asm volatile("ds_read_b64_tr_b16 %0, %1 offset:%2" : "=v"(r) : "v"(addr), "n"(OFF) : "memory");
  return r;
}
struct WF { u32x2 v[4][2]; };
DEV bf16x8 wf_get(const WF& f, int n) {
  u32x4 t; t[0] = f.v[n][0][0]; t[1] = f.v[n][0][1]; t[2] = f.v[n][1][0]; t[3] = f.v[n][1][1];
  return __builtin_bit_cast(bf16x8, t);
}
DEV void wf_wait(WF& f) {
  asm volatile("s_waitcnt lgkmcnt(0)"
               : "+v"(f.v[0][0]), "+v"(f.v[0][1]), "+v"(f.v[1][0]), "+v"(f.v[1][1]), "+v"(f.v[2][0]), "+v"(f.v[2][1]), "+v"(f.v[3][0]), "+v"(f.v[3][1])
               :: "memory");
}
template <int RS01, int RS2> constexpr int ring_off(int r) { return (r & 1) * RS01 + (r >> 1) * RS2; }
template <int T, int RS01, int RS2> DEV void wf_read1(WF& f, const unsigned (&wa)[4], int k) {
  constexpr int o = ring_off<RS01, RS2>(T & 3);
  if (k & 1) f.v[k >> 1][1] = tr_asm<o + 512>(wa[k >> 1]); else f.v[k >> 1][0] = tr_asm<o>(wa[k >> 1]);
}
DEV u32x4 merge16(const u32x2& x, const u32x2& y) {
  const auto r0 = __builtin_amdgcn_permlane16_swap(x[0], y[0], false, false);
  const auto r1 = __builtin_amdgcn_permlane16_swap(x[1], y[1], false, false);
  u32x4 o; o[0] = r0[0]; o[1] = r1[0]; o[2] = r0[1]; o[3] = r1[1];
  return o;
}
template <int OFF> DEV void lds_put(unsigned addr, const f32x4& v) {
  asm volatile("ds_write_b128 %0, %1 offset:%2" ::"v"(addr), "v"(v), "n"(OFF) : "memory");
}
template <int T, int NT, int RS01, int RS2, bool W_IS_A, bool FIRST, int XO, class IW, class IP, class EX>
DEV void work_iter(f32x4 (&acc)[4][4], WF (&wf)[2], u32x4 (&pf)[WD + 2][4], const unsigned (&wa)[4], IW issue_w, IP issue_p, EX extra DIAG_ARGS) {
  constexpr int rem = NT - 2 - T;
  if constexpr (T + 1 < NT) wait_vm<XO + 8 * (rem < WD - 1 ? (rem < 0 ? 0 : rem) : WD - 1)>();
  FENCE();
#pragma unroll
  for (int k = 0; k < 16; ++k) {
    const int a = k & 3, b = 3 - (k >> 2);
    if constexpr (T + 1 < NT) { if (k < 8) wf_read1<T + 1, RS01, RS2>(wf[(T + 1) & 1], wa, k); }
    if constexpr (T + WD + 1 < NT) {
      if ((k & 1) == 0) { if (k < 8) issue_w(T + WD + 1, k >> 1); else pf[(T + WD + 1) % (WD + 2)][(k - 8) >> 1] = issue_p(T + WD + 1, (k - 8) >> 1); }
    }
    const bf16x8 w = wf_get(wf[T & 1], W_IS_A ? a : b);
    const bf16x8 q = __builtin_bit_cast(bf16x8, pf[T % (WD + 2)][W_IS_A ? b : a]);
    const f32x4 c = FIRST ? f32x4{0.f, 0.f, 0.f, 0.f} : acc[a][b];
    acc[a][b] = W_IS_A ? mfma(w, q, c) : mfma(q, w, c);
    FENCE();
    extra(std::integral_constant<int, T>{}, k);
    FENCE();
  }
  if constexpr (T + 1 < NT) wf_wait(wf[(T + 1) & 1]);
  if (DIAGK == 2) STAMP(4 + T);
}
template <int T0, int T1, int NT, int RS01, int RS2, bool W_IS_A, bool FIRST, int XO = 0, class IW, class IP, class EX>
DEV void work_loop(f32x4 (&acc)[4][4], WF (&wf)[2], u32x4 (&pf)[WD + 2][4], const unsigned (&wa)[4], IW issue_w, IP issue_p, EX extra DIAG_ARGS) {
  if constexpr (T0 < T1) {
    work_iter<T0, NT, RS01, RS2, W_IS_A, FIRST, XO>(acc, wf, pf, wa, issue_w, issue_p, extra DIAG_PASS);
    work_loop<T0 + 1, T1, NT, RS01, RS2, W_IS_A, false, XO>(acc, wf, pf, wa, issue_w, issue_p, extra DIAG_PASS);
  }
}
template <int R1, int R2> DEV void put_tile(unsigned xa, const f32x4 (&acc)[4][4], int n) {
  switch (n) {
    case 0: lds_put<R2>(xa, acc[0][3]); break;        case 1: lds_put<R2 + 1024>(xa, acc[1][3]); break;
    case 2: lds_put<R2 + 2048>(xa, acc[2][3]); break; case 3: lds_put<R2 + 3072>(xa, acc[3][3]); break;
    case 4: lds_put<R1>(xa, acc[0][2]); break;        case 5: lds_put<R1 + 1024>(xa, acc[1][2]); break;
    case 6: lds_put<R1 + 2048>(xa, acc[2][2]); break; case 7: lds_put<R1 + 3072>(xa, acc[3][2]); break;
    case 8: lds_put<0>(xa, acc[0][1]); break;         case 9: lds_put<1024>(xa, acc[1][1]); break;
    case 10: lds_put<2048>(xa, acc[2][1]); break;     default: lds_put<3072>(xa, acc[3][1]); break;
  }
}
template <int RS01, int RS2, int XO = 0> DEV void work_first(WF (&wf)[2], const unsigned (&wa)[4]) {
  wait_vm<8 * WD + XO>();
#pragma unroll
  for (int k = 0; k < 8; ++k) wf_read1<0, RS01, RS2>(wf[0], wa, k);
  wf_wait(wf[0]);
}
struct QFrags { bf16x8 w[3], x[5]; };
template <bool RD, bool MM>
DEV void stepq(f32x4 (&acc)[3][5], const QFrags& fo, QFrags& fn, LDSP char* slot, unsigned wo, unsigned xo) {
#pragma unroll
  for (int t = 0; t < 15; ++t) {
    const int mt = t / 3, sec = t - 3 * mt;
    if (RD) {
      if (t < 3) fn.w[t] = tr8(slot + 8192 * t + wo);
      else if (t < 8) fn.x[t - 3] = *(LDSP bf16x8*)(slot + 24576 + 2048 * (t - 3) + xo);
    }
    if (MM && t != 0) acc[sec][mt] = mfma(fo.w[sec], fo.x[mt], acc[sec][mt]);
    FENCE();
  }
}

__global__ __launch_bounds__(512, 1) void k12(const __bf16* __restrict__ x, const __bf16* __restrict__ wqkv,
                                              const __bf16* __restrict__ gw,
                                              const __bf16* __restrict__ w1, const __bf16* __restrict__ b1,
                                              const __bf16* __restrict__ w2, const __bf16* __restrict__ wo,
                                              const __bf16* __restrict__ b2, char* __restrict__ ws,
                                              __bf16* __restrict__ out) {
  extern __shared__ __attribute__((aligned(16))) char smem_g[];
  LDSP char* smem = (LDSP char*)smem_g;
  const int tid = threadIdx.x, lane = tid & 63;
  const int wid = __builtin_amdgcn_readfirstlane(tid >> 6);
  const int xcd = blockIdx.x & 7, jj = blockIdx.x >> 3;
  const int st = (xcd >> 1) * 8 + (jj >> 2), hg = (xcd & 1) * 4 + (jj & 3);
  const int ft = (xcd >> 1) * 7 + (jj >> 2), dt = (xcd & 1) * 4 + (jj & 3);
  const bool mlp = jj < 28;
  const int s0 = st * 64, s8 = s0 + 8 * hg;
  const rsrc_t WSR = mk_rsrc(ws, 8u << 20);
  unsigned tag;
  DIAG_INIT
  if (wid >= 4) {
    const int u = wid - 4;
    const rsrc_t Q = mk_rsrc(wqkv, 512u * 1536u * 2u), X = mk_rsrc(x, (unsigned)S_ * D_ * 2u);
    const unsigned q0 = w_src_off(lane, 0, 3072u), q1 = w_src_off(lane, 1, 3072u);
    const unsigned x0 = (unsigned)(lane >> 3) * 1024u + (unsigned)(((lane & 7) ^ (lane >> 4)) * 16), x1 = x0 ^ 64u;
    const unsigned xh = st == 0 ? 0u : (unsigned)(s0 - 16) * 1024u;
#define ROT 7
    auto issue_q = [&](int s_, int slot) {
      const int s = (s_ + (RQ == 1 ? (jj >> 2) : RQ == 2 ? ((jj >> 2) & 1) * 4 : RQ == 3 ? ((jj >> 2) % 3) * 3 : 0)) & 7;
      if (u < 3) {
#pragma unroll
        for (int j = 0; j < 8; ++j)
          dma16(Q, smem + slot * SLOT + u * 8192 + j * 1024, (j & 1) ? q1 : q0, (unsigned)(u * 1024 + hg * 128) + (unsigned)(64 * s + 8 * j) * 3072u);
      } else {
#pragma unroll
        for (int j = 0; j < 10; ++j)
          dma16(X, smem + slot * SLOT + 24576 + j * 1024, (j & 1) ? x1 : x0, (j < 2 ? xh + j * 8192u : (unsigned)(s0 + 8 * (j - 2)) * 1024u) + s * 128u);
      }
    };
    issue_q(0, 0); issue_q(1, 1); issue_q(2, 2);
    asm volatile("s_load_dword %0, %1, 0x0\n\ts_waitcnt lgkmcnt(0)" : "=s"(tag) : "s"(ws + WS_E) : "memory");
    tag += TAGC;
#define TP 4
    LDSP char* dummy = smem + AX + 16384;
    constexpr int NE = TP == 4 ? 0 : (TP & 1) ? 1 : ((TP & 2) ? 2 : 3), NL = (TP & 1) ? ((TP & 2) ? 1 : 2) : 0;
    unsigned tv[2] = {0u, 0u};
    auto touch_w1 = [&]() {
      const rsrc_t W = mk_rsrc(w1, (unsigned)S_ * F_ * 2u);
      const unsigned r7 = (unsigned)lane / 7u, c7 = (unsigned)lane - r7 * 7u;
      const unsigned tw = lane < 56 ? r7 * (F_ * 2u) + c7 * 128u : 0xfffffff0u;
      if (TP & 2) {
        tv[0] = __builtin_amdgcn_raw_buffer_load_b32(W, tw, (unsigned)(jj * 64 + u * 16 + (xcd & 1) * 8) * (F_ * 2u) + (xcd >> 1) * 896u, 0);
      } else {
        tv[0] = __builtin_amdgcn_raw_buffer_load_b32(W, tw, (unsigned)(jj * 64 + u * 16) * (F_ * 2u) + (xcd >> 1) * 896u, 0);
        tv[1] = __builtin_amdgcn_raw_buffer_load_b32(W, tw, (unsigned)(jj * 64 + u * 16 + 8) * (F_ * 2u) + (xcd >> 1) * 896u, 0);
      }
      FENCE();
    };
    if (TP != 4) {
      const unsigned tq = (lane < 40 && u < 3) ? (unsigned)(lane >> 2) * 3072u + (lane & 3) * 128u : 0xfffffff0u;
      __builtin_amdgcn_raw_ptr_buffer_load_lds(Q, dummy, 4, tq, (unsigned)(192 + jj * 10) * 3072u + u * 1024u + (xcd & 1) * 512u, 0, 0);
      if (!(TP & 1)) touch_w1();
    }
#pragma unroll 1
    for (int s = 0; s < 5; ++s) {
      if (s < 3) { if (u < 3) wait_vm<16 + NE>(); else wait_vm<20 + NE>(); }
      else { if (u < 3) wait_vm<16>(); else wait_vm<20>(); }
      bar();
      issue_q(s + 3, (s + 3) & 3);
    }
    if (TP & 1) touch_w1();
    if (mlp) {
      const int g = lane >> 4, i = lane & 15;
      const rsrc_t W = mk_rsrc(w1, (unsigned)S_ * F_ * 2u), P = mk_rsrc(ws + WS_HT, 2u << 20);
      const unsigned m0 = w_src_off(lane, 0, F_ * 2u), m1 = w_src_off(lane, 1, F_ * 2u);
      const unsigned wb = (unsigned)(ft * 128) + (unsigned)(32 * u) * (F_ * 2u), pb = (unsigned)(dt * 4 * 64 + u) * 1024u;
      LDSP char* ring = smem + u * 8192;
      const int rot = RM == 1 ? 4 * (jj & 3) + 2 * (xcd & 1) : RM == 2 ? 8 * (jj & 1) + 4 * (xcd & 1) : RM == 3 ? 8 * (xcd & 1) : 0;
      auto issue_w = [&](int t, int j) {
        dma16(W, ring + ring_off<4096, SLOT>(t & 3) + j * 1024, (j & 1) ? m1 : m0, wb + (unsigned)(128 * ((t + rot) & 15) + 8 * j) * (F_ * 2u));
      };
      unsigned pj[4];
#pragma unroll
      for (int j = 0; j < 4; ++j) pj[j] = pb + (unsigned)(((j + u) & 3) * 64) * 1024u;
      auto issue_p = [&](int t, int j) -> u32x4 {
        return __builtin_amdgcn_raw_buffer_load_b128(P, lane * 16u, pj[j] + (unsigned)((t + rot) & 15) * 4096u, SC1);
      };
      u32x4 pf[WD + 2][4];
      WF wf[2];
      u32x2 bv[4];
      if (u < 3) wait_vm<16 + NL>(); else wait_vm<20 + NL>();
      bar();
      STAMP(0);
      auto issue = [&](int t) {
#pragma unroll
        for (int j = 0; j < 4; ++j) issue_w(t, j);
        if (PE) {
#pragma unroll
          for (int j = 0; j < 4; ++j) pf[t][j] = issue_p(t, j);
        }
      };
      issue(0); issue(1);
      if (u < 3) wait_vm<16 + 8 * PE + NL>(); else wait_vm<18 + 8 * PE + NL>();
      bar();
      issue(2); issue(3);
      wait_vm<16 + 16 * PE + NL>();
      bar();
#pragma unroll
      for (int a = 0; a < 4; ++a) bv[a] = *(const u32x2*)(b1 + ft * 64 + 16 * a + 4 * g);
      if (!PE) {
#pragma unroll
        for (int j = 0; j < 16; ++j) pf[j >> 2][j & 3] = issue_p(j >> 2, j & 3);
      }
      STAMP(1);
      unsigned wa[4];
#pragma unroll
      for (int n = 0; n < 4; ++n) wa[n] = (unsigned)(uintptr_t)ring + w_lane_off(n, g, i);
      f32x4 acc[4][4];
      const unsigned xa = (unsigned)(uintptr_t)ring + lane * 16u;
      auto extra = [&](auto T, int k) {
        if constexpr (T.value == 15) { if (k >= 4) put_tile<4096, SLOT>(xa, acc, k - 4); }
      };
      STAMP(2);
      work_first<4096, SLOT>(wf, wa);
      sink(tv);
      STAMP(3);
      work_loop<0, 16, 16, 4096, SLOT, true, true>(acc, wf, pf, wa, issue_w, issue_p, extra DIAG_PASS);
      bar();
      STAMP(24);
      f32x4 fin[4];
#pragma unroll
      for (int a = 0; a < 4; ++a) fin[a] = acc[a][0];
#pragma unroll
      for (int k = 1; k < 4; ++k) {
        LDSP char* src = smem + ((u - k) & 3) * 8192 + ring_off<4096, SLOT>(k - 1) + lane * 16;
#pragma unroll
        for (int a = 0; a < 4; ++a) fin[a] += *(LDSP f32x4*)(src + a * 1024);
      }
      u32x2 o[4];
#pragma unroll
      for (int a = 0; a < 4; ++a) {
        float r4[4];
#pragma unroll
        for (int r = 0; r < 4; r += 2) {
          const unsigned c = pack2(fin[a][r], fin[a][r + 1]);
          const unsigned e = pack2(bflo(c) + bflo(bv[a][r >> 1]), bfhi(c) + bfhi(bv[a][r >> 1]));
#pragma unroll
          for (int q = 0; q < 2; ++q) {
            const float uu = q ? bfhi(e) : bflo(e);
            const float z = uu * (1.5957691216057308f + 0.07135481627260025f * uu * uu);
            r4[r + q] = uu - uu * __builtin_amdgcn_rcpf(__expf(z) + 1.f);
          }
        }
        o[a][0] = pack2(r4[0], r4[1]); o[a][1] = pack2(r4[2], r4[3]);
        if (a & 1)
          __builtin_amdgcn_raw_buffer_store_b128(merge16(o[a - 1], o[a]), WSR, (unsigned)((g & 1) * 512 + ((g >> 1) * 16 + i) * 16),
                                                 (unsigned)WS_UG + (unsigned)(((dt * 4 + u) * 56 + 2 * ft + (a >> 1)) * 1024), SC1);
      }
      wait_vm<0>();
      if (lane == 0) __builtin_amdgcn_raw_buffer_store_b32(tag, WSR, 0u, (unsigned)WS_F3 + (unsigned)((dt * 32 + ft) * 4 + u) * 4u, SC1);
      STAMP(25);
      DIAG_DUMP(DIAGK == 2 && tid == 256);
    } else {
      if (u < 3) wait_vm<16 + NL>(); else wait_vm<20 + NL>();
      bar();
      if (u < 3) wait_vm<8 + NL>(); else wait_vm<10 + NL>();
      bar();
      wait_vm<NL>();
      bar();
      const int v = (jj - 28) * 4 + u;
      unsigned b[7], c[1];
      touch<1, 4>(c, mk_rsrc(wo, 512u * 512u * 2u), (unsigned)((xcd >> 1) * 128 + (v & 7) * 16) * (D_ * 2u) + (xcd & 1) * 512u, D_ * 2u, v < 8 ? 64u : 0u, lane);
      touch<7, 8>(b, mk_rsrc(w2, (unsigned)S_ * F_ * 2u), (unsigned)((v * 2 + (xcd & 1)) * 56) * (S_ * 2u) + (xcd >> 1) * 1024u, S_ * 2u, 448u, lane);
      sink(b); sink(c);
      sink(tv);
    }
    return;
  }
  const int g = lane >> 4, i = lane & 15;
  {
    f32x4 acc[3][5];
#pragma unroll
    for (int a = 0; a < 3; ++a)
#pragma unroll
      for (int b = 0; b < 5; ++b) acc[a][b] = f32x4{0.f, 0.f, 0.f, 0.f};
    const unsigned wq = w_lane_off(wid, g, i);
    const unsigned xo0 = (unsigned)(i * 128 + ((g ^ (i >> 1)) * 16)), xo1 = xo0 ^ 64u;
    QFrags fa, fb;
    u32x4 v[2];
#pragma unroll
    for (int r = 0; r < 2; ++r) v[r] = *(const u32x4*)(x + (size_t)(s8 + 2 * wid + r) * D_ + lane * 8);
    asm volatile("" ::: "memory");
    tag = __builtin_amdgcn_readfirstlane(*(const unsigned*)(ws + WS_E) + TAGC);
    {
      float xs[2][8], sum[2], sq[2];
#pragma unroll
      for (int r = 0; r < 2; ++r) {
        sum[r] = 0.f;
#pragma unroll
        for (int e = 0; e < 4; ++e) {
          xs[r][2 * e] = bflo(v[r][e]); xs[r][2 * e + 1] = bfhi(v[r][e]);
          sum[r] += xs[r][2 * e] + xs[r][2 * e + 1];
        }
      }
#pragma unroll
      for (int r = 0; r < 2; ++r) sum[r] = wave_sum(sum[r]) * (1.f / 512.f);
#pragma unroll
      for (int r = 0; r < 2; ++r) {
        sq[r] = 0.f;
#pragma unroll
        for (int e = 0; e < 8; ++e) { const float d = xs[r][e] - sum[r]; sq[r] += d * d; }
      }
      float mb[2], rden[2];
#pragma unroll
      for (int r = 0; r < 2; ++r) {
        mb[r] = rbf(sum[r]);
        const float vb = rbf(wave_sum(sq[r]) * (1.f / 512.f));
        rden[r] = 1.f / rbf(sqrtf(rbf(vb + 1e-5f)));
      }
#pragma unroll
      for (int e = 0; e < 8; ++e) {
        const unsigned c = pack2(xs[0][e] - mb[0], xs[1][e] - mb[1]);
        *(LDSP unsigned*)(smem + AX + lane * 128 + ((e ^ (lane & 7)) * 16) + wid * 4) = pack2(bflo(c) * rden[0], bfhi(c) * rden[1]);
      }
      asm volatile("" : "+s"(tag)::"memory");
      STAMP(0);
      bar();
      STAMP(1);
      const int ks = s8 >> 5, gg = (s8 >> 3) & 3;
#pragma unroll
      for (int k = 0; k < 2; ++k) {
        const int d = k * 256 + tid;
        const u32x4 o = *(LDSP u32x4*)(smem + AX + (((d & ~7) | ((d & 7) ^ ((d >> 3) & 7))) * 16));
        __builtin_amdgcn_raw_buffer_store_b128(o, WSR, (unsigned)((d >> 4) * 65536 + (d & 15) * 16), (unsigned)WS_HT + (unsigned)(ks * 1024 + gg * 256), SC1);
      }
    }
    stepq<true, false>(acc, fb, fa, smem, wq, xo0);
    stepq<true, true>(acc, fa, fb, smem, wq + 4096, xo1);
    auto stage = [&](int s) {
      LDSP char* slot = smem + (s & 3) * SLOT;
      stepq<true, true>(acc, fb, fa, slot, wq, xo0);
      stepq<true, true>(acc, fa, fb, slot, wq + 4096, xo1);
    };
#pragma unroll 1
    for (int s = 1; s < 5; ++s) {
      if (s == 2) wait_vm<0>();
      bar();
      if (s == 2 && tid == 0) __builtin_amdgcn_raw_buffer_store_b32(tag, WSR, blockIdx.x * 4u, (unsigned)WS_F, SC1);
      stage(s);
    }
    STAMP(5);
    bar();
    u32x4 fl = {tag, tag, tag, tag};
    if (wid == 1 && mlp) fl = __builtin_amdgcn_raw_buffer_load_b128(WSR, lane * 16u, (unsigned)WS_F, SC1);
    stage(5);
    STAMP(6);
    bar();
    stage(6);
    STAMP(7);
    while (__builtin_amdgcn_ballot_w64(fl[0] != tag || fl[1] != tag || fl[2] != tag || fl[3] != tag) != 0ull) {
      __builtin_amdgcn_s_sleep(2);
      fl = __builtin_amdgcn_raw_buffer_load_b128(WSR, lane * 16u, (unsigned)WS_F, SC1);
    }
    bar();
    STAMP(8);
    const u32x4 gwv = *(const u32x4*)(gw + lane * 8);
    stage(7);
    stepq<false, true>(acc, fb, fa, smem, wq, xo0);
    STAMP(9);
    LDSP char* kv = smem + AX + wid * 5120;
    LDSP char* qs = wid < 2 ? smem + AX + 20480 + wid * 2048 : smem + (wid - 2) * SLOT + 32768;
#pragma unroll
    for (int mt = 0; mt < 5; ++mt)
#pragma unroll
      for (int sec = 0; sec < 3; ++sec) {
        if (sec == 0 && mt == 0) continue;
        u32x2 o;
        o[0] = pack2(acc[sec][mt][0], acc[sec][mt][1]); o[1] = pack2(acc[sec][mt][2], acc[sec][mt][3]);
        if (sec == 0) *(LDSP u32x2*)(qs + ((g >> 1) * 64 + 16 * (mt - 1) + i) * 16 + 8 * (g & 1)) = o;
        else *(LDSP u32x2*)(kv + (sec - 1) * 2560 + ((g >> 1) * 80 + 16 * mt + i) * 16 + 8 * (g & 1)) = o;
      }
    const int s = s0 + lane;
    const u32x4 q0 = *(LDSP u32x4*)(qs + lane * 16), q1 = *(LDSP u32x4*)(qs + 1024 + lane * 16);
    LDSP char* kr = kv + (lane + 16) * 16;
    float lg[8];
    float mx = -3.0e38f;
#pragma unroll
    for (int w = 0; w < 8; ++w) {
      const u32x4 k0 = *(LDSP u32x4*)(kr - 32 * w), k1 = *(LDSP u32x4*)(kr + 1280 - 32 * w);
      float d = 0.f;
#pragma unroll
      for (int e = 0; e < 4; ++e) {
        d = dot2(q0[e], k0[e], d);
        d = dot2(q1[e], k1[e], d);
      }
      float l = rbf(rbf(d) * 0.25f);
      l = (s - 2 * w >= 0) ? l : -1e9f;
      lg[w] = l;
      mx = fmaxf(mx, l);
    }
    float sum = 0.f;
#pragma unroll
    for (int w = 0; w < 8; ++w) { lg[w] = __expf(lg[w] - mx); sum += lg[w]; }
    const float inv = 1.f / sum;
    float o[16];
#pragma unroll
    for (int e = 0; e < 16; ++e) o[e] = 0.f;
#pragma unroll
    for (int w = 0; w < 8; ++w) {
      const float p = rbf(lg[w] * inv);
      const u32x4 v0 = *(LDSP u32x4*)(kr + 2560 - 32 * w), v1 = *(LDSP u32x4*)(kr + 3840 - 32 * w);
#pragma unroll
      for (int e = 0; e < 4; ++e) {
        o[2 * e] += p * bflo(v0[e]); o[2 * e + 1] += p * bfhi(v0[e]);
        o[8 + 2 * e] += p * bflo(v1[e]); o[8 + 2 * e + 1] += p * bfhi(v1[e]);
      }
    }
    const int h = hg * 4 + wid;
    const unsigned dv = (unsigned)(g * 16384 + i * 16), dso = (unsigned)WS_O + (unsigned)((st * 64 + (h >> 1)) * 1024 + (h & 1) * 512);
    u32x4 a, b;
#pragma unroll
    for (int m = 0; m < 4; ++m) { a[m] = pack2(o[2 * m], o[2 * m + 1]); b[m] = pack2(o[8 + 2 * m], o[8 + 2 * m + 1]); }
    __builtin_amdgcn_raw_buffer_store_b128(a, WSR, dv, dso, SC1);
    __builtin_amdgcn_raw_buffer_store_b128(b, WSR, dv, dso + 256u, SC1);
    float gs[2];
#pragma unroll
    for (int r = 0; r < 2; ++r) {
      gs[r] = 0.f;
#pragma unroll
      for (int e = 0; e < 4; ++e) {
        const unsigned c = pack2(bflo(v[r][e]) * bflo(gwv[e]), bfhi(v[r][e]) * bfhi(gwv[e]));
        gs[r] += bflo(c) + bfhi(c);
      }
      gs[r] = wave_sum(gs[r]);
    }
    if (lane < 2)
      __builtin_amdgcn_raw_buffer_store_b32(ubits(rbf(1.f / (1.f + __expf(-rbf(lane ? gs[1] : gs[0]))))), WSR, lane * 4u, (unsigned)WS_G + (unsigned)(s8 + 2 * wid) * 4u, SC1);
    wait_vm<0>();
    if (lane == 0) __builtin_amdgcn_raw_buffer_store_b32(tag, WSR, 0u, (unsigned)WS_F2 + (unsigned)((st * 8 + hg) * 4 + wid) * 4u, SC1);
    STAMP(10);
  }
  {
    const int u = wid;
    const rsrc_t A = mk_rsrc(wo, 512u * 512u * 2u), B = mk_rsrc(w2, (unsigned)S_ * F_ * 2u), O = mk_rsrc(out, (unsigned)S_ * D_ * 2u);
    const unsigned a0 = w_src_off(lane, 0, D_ * 2u), a1 = w_src_off(lane, 1, D_ * 2u);
    const unsigned c0 = w_src_off(lane, 0, S_ * 2u), c1 = w_src_off(lane, 1, S_ * 2u);
    const unsigned wab = (unsigned)(dt * 128) + (unsigned)(32 * u) * (D_ * 2u), wbb = (unsigned)(st * 128) + (unsigned)(32 * u) * (S_ * 2u);
    const unsigned pab = (unsigned)WS_O + (unsigned)(st * 4 * 16 + u) * 1024u, pbb = (unsigned)WS_UG + (unsigned)(dt * 4 * 56 + u) * 1024u;
    LDSP char* ring = smem + 2 * SLOT + u * 8192;
    LDSP char* rinb = smem + u * 8192;
    const int ra = jj >> 2, rb = RB == 1 ? (((jj & 3) * 2 + (xcd & 1)) * 7) >> 2 : RB == 2 ? (((jj & 1) * 2 + (xcd & 1)) * 7) >> 1 : RB == 3 ? (xcd & 1) * 7 : 0;
    auto effb = [&](int t) { const int e = t + rb; return e >= 14 ? e - 14 : e; };
    auto issue_wa = [&](int t, int j) {
      dma16(A, ring + ring_off<4096, SLOT>(t & 3) + j * 1024, (j & 1) ? a1 : a0, wab + (unsigned)(128 * ((t + ra) & 3) + 8 * j) * (D_ * 2u));
    };
    auto issue_wb = [&](int t, int j) {
      dma16(B, rinb + ring_off<4096, SLOT>(t & 3) + j * 1024, (j & 1) ? c1 : c0, wbb + (unsigned)(128 * effb(t) + 8 * j) * (S_ * 2u));
    };
    auto issue_pa = [&](int t, int j) -> u32x4 {
      return __builtin_amdgcn_raw_buffer_load_b128(WSR, lane * 16u, pab + (unsigned)(((j + u) & 3) * 16) * 1024u + (unsigned)((t + ra) & 3) * 4096u, SC1);
    };
    auto issue_pb = [&](int t, int j) -> u32x4 {
      return __builtin_amdgcn_raw_buffer_load_b128(WSR, lane * 16u, pbb + (unsigned)(j * 56) * 1024u + (unsigned)effb(t) * 4096u, SC1);
    };
    auto pload = [&](unsigned off, int n) {
      u32x4 f = {tag, tag, tag, tag};
      if (lane < n) f = __builtin_amdgcn_raw_buffer_load_b128(WSR, lane * 16u, off, SC1);
      return f;
    };
    auto pbad = [&](const u32x4& f) { return __builtin_amdgcn_ballot_w64(f[0] != tag || f[1] != tag || f[2] != tag || f[3] != tag) != 0ull; };
    auto pwait = [&](const u32x4& f0, unsigned off, int n) {
      bool bad = pbad(f0);
      FENCE();
      while (bad) {
        asm volatile("" ::: "memory");
        __builtin_amdgcn_s_sleep(16);
        bad = pbad(pload(off, n));
      }
      FENCE();
    };
    u32x4 pf[WD + 2][4];
    WF wf[2];
    unsigned wa[4], wb[4];
#pragma unroll
    for (int n = 0; n < 4; ++n) {
      wa[n] = (unsigned)(uintptr_t)ring + w_lane_off(n, g, i);
      wb[n] = (unsigned)(uintptr_t)rinb + w_lane_off((n + u) & 3, g, i);
    }
    const int so = st * 64 + 16 * u + i;
    const float bb = (float)b2[so];
    if (mlp) bar();
    bar();
    STAMP(14);
    const u32x4 f2 = pload((unsigned)WS_F2 + (unsigned)st * 128u, 8);
    FENCE();
#pragma unroll
    for (int j = 0; j < 16; ++j) issue_wa(j >> 2, j & 3);
    pwait(f2, (unsigned)WS_F2 + (unsigned)st * 128u, 8);
    STAMP(11);
#pragma unroll
    for (int j = 0; j < 16; ++j) pf[j >> 2][j & 3] = issue_pa(j >> 2, j & 3);
    const float gt = fbits(__builtin_amdgcn_raw_buffer_load_b32(WSR, (unsigned)so * 4u, (unsigned)WS_G, SC1));
    FENCE();
    const u32x4 f3 = pload((unsigned)WS_F3 + (unsigned)dt * 512u, 28);
    FENCE();
#pragma unroll
    for (int j = 0; j < 16; ++j) issue_wb(j >> 2, j & 3);
    f32x4 acc[4][4];
    auto none = [&](auto, int) {};
    work_first<4096, SLOT, 17>(wf, wa);
    work_loop<0, 4, 4, 4096, SLOT, true, true, 17>(acc, wf, pf, wa, issue_wa, issue_pa, none DIAG_PASS);
    STAMP(12);
    pwait(f3, (unsigned)WS_F3 + (unsigned)dt * 512u, 28);
    STAMP(13);
#pragma unroll
    for (int j = 0; j < 16; ++j) pf[j >> 2][j & 3] = issue_pb(j >> 2, j & 3);
    const unsigned xa = (unsigned)(uintptr_t)ring + lane * 16u, xb = (unsigned)(uintptr_t)rinb + lane * 16u;
#pragma unroll
    for (int n = 0; n < 12; ++n) put_tile<4096, SLOT>(xa, acc, n);
    f32x4 fa[4];
#pragma unroll
    for (int a = 0; a < 4; ++a) fa[a] = acc[a][0];
    auto extra = [&](auto T, int k) {
      if constexpr (T.value == 13) { if (k >= 4) put_tile<4096, SLOT>(xb, acc, k - 4); }
    };
    work_first<4096, SLOT>(wf, wb);
    work_loop<0, 14, 14, 4096, SLOT, false, true>(acc, wf, pf, wb, issue_wb, issue_pb, extra DIAG_PASS);
    STAMP(24);
    bar();
    STAMP(25);
    f32x4 fb[4];
#pragma unroll
    for (int a = 0; a < 4; ++a) fb[a] = acc[a][0];
#pragma unroll
    for (int k = 1; k < 4; ++k) {
      LDSP char* src = smem + ((u - k) & 3) * 8192 + ring_off<4096, SLOT>(k - 1) + lane * 16;
#pragma unroll
      for (int a = 0; a < 4; ++a) { fb[a] += *(LDSP f32x4*)(src + a * 1024); fa[a] += *(LDSP f32x4*)(src + 2 * SLOT + a * 1024); }
    }
    const float omg = rbf(1.f - gt);
    const unsigned oo = (unsigned)(so * D_ + dt * 64 + 16 * (g & 1) + 8 * (g >> 1)) * 2u;
    u32x2 o[4];
#pragma unroll
    for (int a = 0; a < 4; ++a) {
      float r4[4];
#pragma unroll
      for (int r = 0; r < 4; r += 2) {
        const unsigned pa = pack2(fa[a][r], fa[a][r + 1]), pb = pack2(fb[a][r], fb[a][r + 1]);
        const unsigned pc = pack2(bflo(pb) + bb, bfhi(pb) + bb);
        const unsigned ga = pack2(gt * bflo(pa), gt * bfhi(pa)), gb = pack2(omg * bflo(pc), omg * bfhi(pc));
        r4[r] = bflo(ga) + bflo(gb); r4[r + 1] = bfhi(ga) + bfhi(gb);
      }
      o[a][0] = pack2(r4[0], r4[1]); o[a][1] = pack2(r4[2], r4[3]);
      if (a & 1) __builtin_amdgcn_raw_buffer_store_b128(merge16(o[a - 1], o[a]), O, oo, 64 * (a >> 1), 0);
    }
    if (blockIdx.x == 0 && tid == 0) *(unsigned*)(ws + WS_E) = tag;
    STAMP(26);
    DIAG_DUMP(DIAGK == 1 && tid == 0);
  }
}

extern "C" void kernel_launch(void* const* d_in, const int* in_sizes, int n_in, void* d_out, int out_size,
                              void* d_ws, size_t ws_size, hipStream_t stream) {
  const __bf16* x = (const __bf16*)d_in[0];
  const __bf16* wqkv = (const __bf16*)d_in[1];
  const __bf16* wo = (const __bf16*)d_in[2];
  const __bf16* w1 = (const __bf16*)d_in[4];
  const __bf16* b1 = (const __bf16*)d_in[5];
  const __bf16* w2 = (const __bf16*)d_in[6];
  const __bf16* b2 = (const __bf16*)d_in[7];
  const __bf16* gw = (const __bf16*)d_in[8];
  char* ws = (char*)d_ws;
  (void)hipFuncSetAttribute((const void*)k12, hipFuncAttributeMaxDynamicSharedMemorySize, LDS_BYTES);
  k12<<<256, 512, LDS_BYTES, stream>>>(x, wqkv, gw, w1, b1, w2, wo, b2, ws, (__bf16*)d_out);
}
```
